```python
import torch

def _softmax(x, axis=-1):
    return torch.softmax(x, dim=axis)

def _gelu(x):
    return torch.nn.functional.gelu(x, approximate='tanh')

def _silu(x):
    return torch.nn.functional.silu(x)

def _sigmoid(x):
    return torch.sigmoid(x)

def _layernorm(x, axis=-1, eps=1e-05):
    m = x.mean(dim=axis, keepdim=True)
    v = x.var(dim=axis, keepdim=True, unbiased=False)
    return (x - m) / torch.sqrt(v + eps)

def _scatter_add(tgt, idx, src, axis=0):
    return tgt.clone().index_add_(axis, idx, src)

def _segment_sum(src, seg, n):
    out = torch.zeros((n,) + src.shape[1:], dtype=src.dtype)
    return out.index_add_(0, seg, src)

def _pad(x, pw):
    flat = tuple((v for pair in reversed(pw) for v in pair))
    return torch.nn.functional.pad(x, flat)
kernel_name = 'gated_mix__memory_aug_recurrent_mem_tr__ssm_conv_s4__prompt'

def setup_inputs():

    _w = lambda a: (lambda t: t.to(torch.bfloat16) if t.is_floating_point() else t)(torch.as_tensor(a))

    def _A_setup():
        B, S, Dm = (1, 2048, 512)
        H, Dh = (32, 128)
        M, seg = (8, 32)
        seg = min(seg, S)
        S = S // seg * seg
        return {'x': _w((lambda *s: torch.randn(*s, dtype=torch.bfloat16) * 0.05)(B, S, Dm)), 'mem0': _w((lambda *s: torch.randn(*s, dtype=torch.bfloat16) * 0.4)(B, M, Dm)), 'Wq': _w((lambda *s: torch.randn(*s, dtype=torch.bfloat16) * 6.4)(Dm, H, Dh) * Dm ** (-0.5)), 'Wk': _w((lambda *s: torch.randn(*s, dtype=torch.bfloat16) * 6.4)(Dm, H, Dh) * Dm ** (-0.5)), 'Wv': _w((lambda *s: torch.randn(*s, dtype=torch.bfloat16) * 0.4)(Dm, H, Dh) * Dm ** (-0.5)), 'Wo': _w((lambda *s: torch.randn(*s, dtype=torch.bfloat16) * 0.4)(H, Dh, Dm) * (H * Dh) ** (-0.5)), 'seg_len': seg}
    _A_in = _A_setup()

    def _B_setup():
        B, L, D, N = (1, 2048, 512, 128)
        A_re = -(lambda _x: torch.exp(torch.as_tensor(_x)))((lambda *s: torch.randn(*s, dtype=torch.bfloat16) * 6.4)(D, N))
        A_im = torch.pi * (lambda *s: torch.randn(*s, dtype=torch.bfloat16) * 0.05)(D, N)
        B_re = (lambda *s: torch.randn(*s, dtype=torch.bfloat16) * 0.1)(D, N)
        B_im = (lambda *s: torch.randn(*s, dtype=torch.bfloat16) * 0.1)(D, N)
        C_re = (lambda *s: torch.randn(*s, dtype=torch.bfloat16) * 0.1)(D, N)
        C_im = (lambda *s: torch.randn(*s, dtype=torch.bfloat16) * 0.1)(D, N)
        log_dt = (lambda *s: torch.randn(*s, dtype=torch.bfloat16) * 0.05)(D) * 0.5 - 4.0
        Dskip = (lambda *s: torch.randn(*s, dtype=torch.bfloat16) * 0.05)(D)
        return {'u': _w((lambda *s: torch.randn(*s, dtype=torch.bfloat16) * 0.05)(B, L, D)), 'A_re': _w(A_re), 'A_im': _w(A_im), 'B_re': _w(B_re), 'B_im': _w(B_im), 'C_re': _w(C_re), 'C_im': _w(C_im), 'log_dt': _w(log_dt), 'Dskip': _w(Dskip)}
    _B_in = _B_setup()
    return {'A_x': _A_in['x'], 'A_mem0': _A_in['mem0'], 'A_Wq': _A_in['Wq'], 'A_Wk': _A_in['Wk'], 'A_Wv': _A_in['Wv'], 'A_Wo': _A_in['Wo'], 'A_seg_len': _A_in['seg_len'], 'B_u': _B_in['u'], 'B_A_re': _B_in['A_re'], 'B_A_im': _B_in['A_im'], 'B_B_re': _B_in['B_re'], 'B_B_im': _B_in['B_im'], 'B_C_re': _B_in['C_re'], 'B_C_im': _B_in['C_im'], 'B_log_dt': _B_in['log_dt'], 'B_Dskip': _B_in['Dskip'], 'gate_w': _w((lambda *s: torch.randn(*s, dtype=torch.bfloat16) * 0.05)(512) * 16.0)}

def reference(A_x, A_mem0, A_Wq, A_Wk, A_Wv, A_Wo, A_seg_len, B_u, B_A_re, B_A_im, B_B_re, B_B_im, B_C_re, B_C_im, B_log_dt, B_Dskip, gate_w):

    def _A_ref(x, mem0, Wq, Wk, Wv, Wo, seg_len):
        B, S, Dm = x.shape
        H, Dh = (Wq.shape[1], Wq.shape[2])
        M = mem0.shape[1]
        scale = Dh ** (-0.5)
        n_seg = S // seg_len
        L = M + seg_len
        idx = torch.arange(L)
        mask = (idx[:, None] < M) | (idx[:, None] >= idx[None, :])
        mem = mem0
        outs = []
        for s in range(n_seg):
            xs = x[:, s * seg_len:(s + 1) * seg_len, :].clone()
            h = torch.concatenate([mem, xs], axis=1)
            q = torch.einsum('btd,dhe->bhte', h, Wq)
            k = torch.einsum('btd,dhe->bhte', h, Wk)
            v = torch.einsum('btd,dhe->bhte', h, Wv)
            scores = torch.einsum('bhqe,bhke->bhqk', q, k) * scale
            scores = torch.where(mask[None, None], scores, -1000000000.0)
            p = _softmax(scores, axis=-1)
            o = torch.einsum('bhqk,bhke->bhqe', p, v)
            o = torch.einsum('bhte,hed->btd', o, Wo)
            mem = o[:, :M, :].clone()
            outs.append(o[:, M:, :])
        return torch.concatenate(outs, axis=1)

    def _B_ref(u, A_re, A_im, B_re, B_im, C_re, C_im, log_dt, Dskip):
        Bsz, L, D = u.shape
        dt = torch.exp(log_dt)[:, None]
        A = A_re + 1j * A_im
        Bc = B_re + 1j * B_im
        Cc = C_re + 1j * C_im
        logdA = dt * A
        dB = dt * Bc
        l = (lambda x, dt: x.to(dt))(torch.arange(L), A_re.dtype)
        P = torch.exp(logdA[:, :, None] * l[None, None, :])
        K = torch.einsum('dn,dnl,dn->dl', Cc, P, dB).real
        L2 = 2 * L
        Kp = _pad(K, ((0, 0), (0, L)))
        up = (lambda x, *d: x.permute(*d))(u, 0, 2, 1)
        up = _pad(up, ((0, 0), (0, 0), (0, L)))
        Kf = (lambda x, axes=(-2, -1): torch.fft.rfft2(x.float(), dim=axes))(Kp[:, None, :], axes=(-2, -1))
        Uf = (lambda x, axes=(-2, -1): torch.fft.rfft2(x.float(), dim=axes))(up[:, :, None, :], axes=(-2, -1))
        Yf = Uf * Kf[None, :, :, :]
        y = (lambda x, s, axes=(-2, -1): torch.fft.irfft2(x, s=s, dim=axes).to(torch.bfloat16))(Yf, (1, L2), axes=(-2, -1))[:, :, 0, :L]
        y = (lambda x, *d: x.permute(*d))(y, 0, 2, 1) + u * Dskip[None, None, :]
        return y
    x = A_x
    g = _sigmoid((x * gate_w).sum(-1, keepdims=True))
    a = _A_ref(x, A_mem0, A_Wq, A_Wk, A_Wv, A_Wo, A_seg_len)
    b = _B_ref(x, B_A_re, B_A_im, B_B_re, B_B_im, B_C_re, B_C_im, B_log_dt, B_Dskip)
    return g * a + (1 - g) * b
```

```cpp
#include <hip/hip_runtime.h>
#include <hip/hip_bf16.h>
#include <stdint.h>

typedef __bf16 bf8 __attribute__((ext_vector_type(8)));
typedef __bf16 bf16v __attribute__((ext_vector_type(16)));
typedef float f4 __attribute__((ext_vector_type(4)));
typedef float f16v __attribute__((ext_vector_type(16)));
typedef unsigned u4 __attribute__((ext_vector_type(4)));
typedef unsigned u2 __attribute__((ext_vector_type(2)));
typedef unsigned u8v __attribute__((ext_vector_type(8)));
typedef unsigned short u16;
#define DEVI __device__ __forceinline__
#define GAS __attribute__((address_space(1)))
#define LAS __attribute__((address_space(3)))

constexpr int S_ = 2048, DM = 512, NH = 32, DH = 128, NSEG = 64, NST = 128;
constexpr unsigned SENT = 0xFFFFFFFFu;
constexpr size_t MB = 1u << 20;
constexpr size_t O_MEM = 0;
constexpr size_t O_MEM2 = 512 * 1024;
#define NREP 1
constexpr size_t O_PM  = 160 * MB;
constexpr size_t O_FLG = 1 * MB;
constexpr int NSPLIT = 3;
constexpr int F_A = 0, F_AP = 4096, F_S4 = 8192, F_PX = 16384, F_Z = 32768, F_F = 49152, FLG_BYTES = 65536;
constexpr size_t O_G   = 2 * MB;
constexpr size_t O_B   = 3 * MB;
constexpr size_t O_XT  = 5 * MB;
constexpr size_t O_WOT = 7 * MB;
constexpr size_t O_WT  = 11 * MB;
constexpr size_t O_FT  = 23 * MB;
constexpr size_t O_KX  = 39 * MB;
constexpr size_t O_QX  = 55 * MB;
constexpr size_t O_ZM  = 71 * MB;
constexpr size_t O_PXX = 87 * MB;
constexpr size_t O_PXM = 91 * MB;
constexpr size_t O_UX  = 92 * MB;
constexpr int PMSTEP = 20480, MEMSTEP = 8192;

struct Params {
    const u16 *x, *mem0, *wq, *wk, *wv, *wo, *are, *aim, *bre, *bim, *cre, *cim, *logdt, *dskip, *gw;
    u16* out; char* ws;
};

DEVI float bf2f(u16 v) { return __uint_as_float((unsigned)v << 16); }
DEVI unsigned pk2(float lo, float hi) {
    typedef __bf16 b2 __attribute__((ext_vector_type(2)));
    b2 r; r[0] = (__bf16)lo; r[1] = (__bf16)hi; return __builtin_bit_cast(unsigned, r);
}
DEVI float rbf(float v) { return (float)(__bf16)v; }
DEVI u16 f2bf(float v) { return __builtin_bit_cast(u16, (__bf16)v); }
DEVI unsigned nosent(unsigned v) { return min(v, 0xFFFFFFFEu); }
DEVI int dmap(int k) { return 16 * (k >> 4) + 8 * ((k >> 1) & 1) + 2 * ((k >> 2) & 3) + (k & 1); }
DEVI bf8 asbf8(u4 v) { return __builtin_bit_cast(bf8, v); }
DEVI f4 mfma16(u4 a, u4 b, f4 c) { return __builtin_amdgcn_mfma_f32_16x16x32_bf16(asbf8(a), asbf8(b), c, 0, 0, 0); }
DEVI f4 smfma(u4 a, u8v b, f4 c, int idx) { return __builtin_amdgcn_smfmac_f32_16x16x64_bf16(asbf8(a), __builtin_bit_cast(bf16v, b), c, idx, 0, 0); }
DEVI f16v mfma32(u4 a, u4 b, f16v c) { return __builtin_amdgcn_mfma_f32_32x32x16_bf16(asbf8(a), asbf8(b), c, 0, 0, 0); }
#define RAW_BARRIER() do { asm volatile("s_waitcnt lgkmcnt(0)" ::: "memory"); __builtin_amdgcn_s_barrier(); asm volatile("" ::: "memory"); } while (0)
DEVI void st16_sc1(char* base, int off, u4 v) { asm volatile("s_nop 4\n\tglobal_store_dwordx4 %0, %1, %2 sc1\n\ts_nop 1" :: "v"(off), "v"(v), "s"(base) : "memory"); }
DEVI void st8_sc1(char* base, int off, u2 v) { asm volatile("s_nop 4\n\tglobal_store_dwordx2 %0, %1, %2 sc1\n\ts_nop 1" :: "v"(off), "v"(v), "s"(base) : "memory"); }
DEVI void st4_sc1(char* base, int off, unsigned v) { asm volatile("s_nop 4\n\tglobal_store_dword %0, %1, %2 sc1" :: "v"(off), "v"(v), "s"(base) : "memory"); }
constexpr unsigned SPIN_CAP = 1u << 16;
template <int SLP>
DEVI void wait_words(const char* base, int off) {
    for (unsigned spins = 0; spins < SPIN_CAP; ++spins) {
        unsigned v;
        asm volatile("s_nop 4\n\tglobal_load_dword %0, %1, %2 sc1\n\ts_waitcnt vmcnt(0)" : "=v"(v) : "v"(off), "s"(base) : "memory");
        if (__builtin_amdgcn_ballot_w64(v == SENT) == 0) break;
        __builtin_amdgcn_s_sleep(SLP);
    }
}
#define VM0() asm volatile("s_waitcnt vmcnt(0)" ::: "memory")
DEVI unsigned umax3(unsigned a, unsigned b, unsigned c) { return max(max(a, b), c); }
DEVI unsigned mx4(unsigned m, u4 v) { return umax3(umax3(m, v.x, v.y), v.z, v.w); }
DEVI float swap32_add(float a, float b) {
    auto r = __builtin_amdgcn_permlane32_swap(__float_as_uint(a), __float_as_uint(b), false, false);
    return __uint_as_float(r[0]) + __uint_as_float(r[1]);
}
DEVI float gmax(float v) {
    auto r = __builtin_amdgcn_permlane32_swap(__float_as_uint(v), __float_as_uint(v), false, false);
    v = fmaxf(__uint_as_float(r[0]), __uint_as_float(r[1]));
    auto q = __builtin_amdgcn_permlane16_swap(__float_as_uint(v), __float_as_uint(v), false, false);
    return fmaxf(__uint_as_float(q[0]), __uint_as_float(q[1]));
}
DEVI float gsum(float v) {
    auto r = __builtin_amdgcn_permlane32_swap(__float_as_uint(v), __float_as_uint(v), false, false);
    v = __uint_as_float(r[0]) + __uint_as_float(r[1]);
    auto q = __builtin_amdgcn_permlane16_swap(__float_as_uint(v), __float_as_uint(v), false, false);
    return __uint_as_float(q[0]) + __uint_as_float(q[1]);
}

DEVI u4 pack8(f4 a, f4 b) { return u4{pk2(a[0], a[1]), pk2(a[2], a[3]), pk2(b[0], b[1]), pk2(b[2], b[3])}; }
DEVI void unitF(const Params& p, char* sm, int h, int cq, int dh) {
    const int tid = threadIdx.x, l = tid & 63, w = tid >> 6, g = l >> 4, m = l & 15, k64 = dh * 4 + w;
    u4 r[8], vf[2][2][4];
    {
        const u16* src = p.wo + (size_t)h * 128 * 512 + cq * 128 + m * 8;
#pragma unroll
        for (int j = 0; j < 8; ++j) r[j] = *(const u4*)(src + (32 * w + (j < 4 ? 4 * g + j : 12 + 4 * g + j)) * 512);
    }
#pragma unroll
    for (int ha = 0; ha < 4; ++ha) {
        const u16* arow = p.wv + (size_t)(64 * k64 + dmap(32 * (ha >> 1) + 8 * (m >> 2) + 4 * (ha & 1) + (m & 3))) * 4096 + h * 128 + 4 * g;
#pragma unroll
        for (int ks = 0; ks < 4; ++ks) { u2 lo = *(const u2*)(arow + 32 * ks), hi = *(const u2*)(arow + 32 * ks + 16); vf[ha >> 1][ha & 1][ks] = u4{lo.x, lo.y, hi.x, hi.y}; }
    }
    const int di = (((m >> 1) * 4 + w) * 64 + g * 16 + (m & 1) * 8) * 16;
#pragma unroll
    for (int i = 0; i < 8; ++i) {
        u4 o;
#pragma unroll
        for (int j = 0; j < 4; ++j) o[j] = __builtin_amdgcn_perm(r[2 * j + 1][i >> 1], r[2 * j][i >> 1], (i & 1) ? 0x07060302u : 0x05040100u);
        *(u4*)(sm + di + i * 16) = o;
        if (dh == 0) *(u4*)(p.ws + O_WOT + (size_t)(h * 4 + cq) * 32768 + di + i * 16) = o;
    }
    __syncthreads();
    char* fb = p.ws + O_FT + (size_t)(((h >> 3) * 4 + ((h >> 1) & 3)) * 8 + k64) * 2048 + (size_t)(cq * 16 + (m >> 3)) * 262144 + (g * 16 + (h & 1) * 8 + (m & 7)) * 16;
#pragma unroll
    for (int ct = 0; ct < 8; ++ct) {
        u4 wv[4];
#pragma unroll
        for (int ks = 0; ks < 4; ++ks) wv[ks] = *(const u4*)(sm + (ct * 4 + ks) * 1024 + l * 16);
#pragma unroll
        for (int hf = 0; hf < 2; ++hf) {
            f4 a0 = {0, 0, 0, 0}, a1 = {0, 0, 0, 0};
#pragma unroll
            for (int ks = 0; ks < 4; ++ks) { a0 = mfma16(vf[hf][0][ks], wv[ks], a0); a1 = mfma16(vf[hf][1][ks], wv[ks], a1); }
            *(u4*)(fb + (size_t)ct * 524288 + hf * 1024) = pack8(a0, a1);
        }
    }
}
constexpr int NCH_WT = 3 * 32 * 128 * 64, NCH_WOT = 32 * 128 * 64, NCH_XT = 64 * 32 * 64;
constexpr int NT_WT = NCH_WT / 8, NT_WOT = 0;
DEVI void retile(const Params& p, int t) {
    if (t >= NT_WT + NT_WOT) {
        int i2 = t - NT_WT - NT_WOT; int l = i2 & 63, f = i2 >> 6, kk = f & 15, a = (f >> 4) & 1, s = f >> 5, m = l & 15, g = l >> 4;
        int tok = 32 * s + 8 * (m >> 2) + 4 * a + (m & 3);
        const u16* src = p.x + (size_t)tok * 512 + 32 * kk + 16 * (g >> 1) + 4 * (g & 1);
        u2 lo = *(const u2*)src, hi = *(const u2*)(src + 8);
        *(u4*)(p.ws + O_XT + (size_t)i2 * 16) = u4{lo.x, hi.x, lo.y, hi.y};
        return;
    }
    u4 r[8]; char* dst;
    {
        int mh = t & 1, et = (t >> 1) & 7, g = (t >> 4) & 3, half = (t >> 6) & 1, ks = (t >> 7) & 7, h = (t >> 10) & 31, mat = t >> 15;
        const u16* src = (mat == 0 ? p.wq : mat == 1 ? p.wk : p.wv) + h * 128 + et * 16 + mh * 8;
#pragma unroll
        for (int j = 0; j < 8; ++j) r[j] = *(const u4*)(src + (size_t)(64 * ks + dmap(32 * half + 8 * g + j)) * 4096);
        dst = p.ws + O_WT + ((size_t)((((mat * 32 + h) * 8 + et) * 8 + ks) * 2 + half) * 64 + g * 16 + mh * 8) * 16;
    }
#pragma unroll
    for (int i = 0; i < 8; ++i) {
        u4 o;
#pragma unroll
        for (int j = 0; j < 4; ++j) o[j] = __builtin_amdgcn_perm(r[2 * j + 1][i >> 1], r[2 * j][i >> 1], (i & 1) ? 0x07060302u : 0x05040100u);
        *(u4*)(dst + i * 16) = o;
    }
}
DEVI void gate_rows(const Params& p, int blk) {
    const int l = threadIdx.x & 63, w = threadIdx.x >> 6;
    u4 gw = *(const u4*)(p.gw + l * 8);
    u4 xvv[4];
#pragma unroll
    for (int i = 0; i < 4; ++i) xvv[i] = *(const u4*)(p.x + (size_t)(blk * 16 + w * 4 + i) * 512 + l * 8);
#pragma unroll
    for (int i = 0; i < 4; ++i) {
        int t = blk * 16 + w * 4 + i;
        u4 xv = xvv[i];
        float s = 0.f;
#pragma unroll
        for (int j = 0; j < 4; ++j) {
            s += rbf(__uint_as_float(xv[j] << 16) * __uint_as_float(gw[j] << 16));
            s += rbf(__uint_as_float(xv[j] & 0xFFFF0000u) * __uint_as_float(gw[j] & 0xFFFF0000u));
        }
#pragma unroll
        for (int o = 32; o > 0; o >>= 1) s += __shfl_xor(s, o);
        s = rbf(s);
        float gv = rbf(1.f / (1.f + __expf(-s)));
        if (l == 0) ((float*)(p.ws + O_G))[t] = gv;
    }
}
constexpr int PRE_F = 256, PRE_G = 128, PRE_S = 64, PRE_RT = (NT_WT + NT_WOT + NCH_XT) / 256;
__global__ void __launch_bounds__(256) k_pre(Params p) {
    int b = blockIdx.x;
    __shared__ __attribute__((aligned(16))) char sm[32768];
    if (b < PRE_F) { unitF(p, sm, (b & 7) | ((b >> 6) << 3), (b >> 3) & 3, (b >> 5) & 1); return; }
    b -= PRE_F;
    if (b < PRE_G) {
        gate_rows(p, b);
        if (b == 0) {
            for (int i = threadIdx.x; i < 512; i += 256) {
                int cb = i >> 3, tok = i & 7;
                u4 v = *(const u4*)(p.mem0 + tok * 512 + cb * 8);
                v.x = nosent(v.x); v.y = nosent(v.y); v.z = nosent(v.z); v.w = nosent(v.w);
                *(u4*)(p.ws + O_MEM + i * 16) = v; *(u4*)(p.ws + O_MEM2 + i * 16) = v;
            }
        }
        return;
    }
    b -= PRE_G;
    if (b < PRE_S) {
        const u4 sv = {SENT, SENT, SENT, SENT};
        constexpr int N1 = (int)((O_FLG + FLG_BYTES) / 16), N2 = 64 * NREP * PMSTEP / 16;
        for (int i = b * 256 + threadIdx.x; i < N1 + N2; i += PRE_S * 256) {
            if (i >= N1) *(u4*)(p.ws + O_PM + (size_t)(i - N1) * 16) = sv;
            else if (i >= 512 && (i < (int)(O_MEM2 / 16) || i >= (int)(O_MEM2 / 16) + 512)) *(u4*)(p.ws + (size_t)i * 16) = sv;
        }
        return;
    }
    b -= PRE_S;
    retile(p, b * 256 + threadIdx.x);
}

DEVI void ring_fill(const Params& p, char* smem, int h, int c, int w, int l) {
    const char* src = c < 24 ? p.ws + O_WT + ((size_t)((c >> 3) * 32 + h) * 8 + (c & 7)) * 16384
                             : p.ws + O_WOT + (size_t)h * 131072 + (size_t)(c - 24) * 16384;
    char* dst = smem + (c & 7) * 16384 + w * 4096;
#pragma unroll
    for (int i = 0; i < 4; ++i)
        __builtin_amdgcn_global_load_lds((const GAS void*)(src + w * 4096 + i * 1024 + l * 16), (LAS void*)(dst + i * 1024), 16, 0, 0);
}
DEVI void projtile(const char* slot, const u4 (&xf)[2][16], f4& a0, f4& a1, int l) {
    a0 = f4{0, 0, 0, 0}; a1 = f4{0, 0, 0, 0};
    u4 wf[16];
    __builtin_amdgcn_sched_barrier(0);
#pragma unroll
    for (int kk = 0; kk < 16; ++kk) wf[kk] = *(const u4*)(slot + kk * 1024 + l * 16);
    __builtin_amdgcn_sched_barrier(0);
#pragma unroll
    for (int kk = 0; kk < 16; ++kk) {
        a0 = mfma16(wf[kk], xf[0][kk], a0);
        a1 = mfma16(wf[kk], xf[1][kk], a1);
    }
}
#define RING_SYNC(N) do { asm volatile("s_waitcnt vmcnt(" #N ")" ::: "memory"); RAW_BARRIER(); } while (0)
template <bool SC1>
DEVI void st16(char* base, int off, u4 v) { if (SC1) st16_sc1(base, off, v); else *(u4*)(base + off) = v; }
template <bool SC1>
DEVI void unitA(const Params& p, char* smem, int sg, int h) {
    const int tid = threadIdx.x, l = tid & 63, w = __builtin_amdgcn_readfirstlane(tid >> 6), g = l >> 4, m = l & 15;
    const int s = sg * 4 + w;
    RAW_BARRIER();
    u4 xf[2][16];
#pragma unroll
    for (int a = 0; a < 2; ++a)
#pragma unroll
        for (int kk = 0; kk < 16; ++kk) xf[a][kk] = *(const u4*)(p.ws + O_XT + ((size_t)(s * 2 + a) * 16 + kk) * 1024 + l * 16);
#pragma unroll
    for (int c = 0; c < 7; ++c) ring_fill(p, smem, h, c, w, l);
    RING_SYNC(24);
    f4 e0, e1, o0, o1;
    for (int pr = 0; pr < 8; ++pr) {
        int c = 2 * pr;
        ring_fill(p, smem, h, c + 7, w, l);
        projtile(smem + (c & 7) * 16384, xf, e0, e1, l);
        RING_SYNC(24);
        ring_fill(p, smem, h, c + 8, w, l);
        projtile(smem + ((c + 1) & 7) * 16384, xf, o0, o1, l);
        char* dst = p.ws + (pr < 4 ? O_QX : O_KX) + (size_t)(s * 32 + h) * 8192;
        st16<SC1>(dst, (pr & 3) * 1024 + l * 16, pack8(e0, o0));
        st16<SC1>(dst, 4096 + (pr & 3) * 1024 + l * 16, pack8(e1, o1));
        RING_SYNC(24);
    }
    u4 vf[2][4];
#pragma unroll
    for (int pr = 0; pr < 4; ++pr) {
        int c = 16 + 2 * pr;
        ring_fill(p, smem, h, c + 7, w, l);
        projtile(smem + (c & 7) * 16384, xf, e0, e1, l);
        RING_SYNC(24);
        ring_fill(p, smem, h, c + 8, w, l);
        projtile(smem + ((c + 1) & 7) * 16384, xf, o0, o1, l);
        vf[0][pr] = pack8(e0, o0); vf[1][pr] = pack8(e1, o1);
        RING_SYNC(24);
    }
    char* ubase = p.ws + O_UX + (size_t)s * 64 * 16384 + (size_t)((h >> 3) * 4 + ((h >> 1) & 3)) * 1024;
    const int uoff = (m >> 3) * 16384 + (g * 16 + (h & 1) * 8 + (m & 7)) * 16;
#pragma unroll
    for (int c = 24; c < 32; ++c) {
        if (c + 7 < 32) ring_fill(p, smem, h, c + 7, w, l);
        const char* slot = smem + (c & 7) * 16384;
        u4 wv[16];
        __builtin_amdgcn_sched_barrier(0);
#pragma unroll
        for (int kk = 0; kk < 16; ++kk) wv[kk] = *(const u4*)(slot + kk * 1024 + l * 16);
        __builtin_amdgcn_sched_barrier(0);
#pragma unroll
        for (int q = 0; q < 4; ++q) {
            f4 a0 = {0, 0, 0, 0}, a1 = {0, 0, 0, 0};
#pragma unroll
            for (int ks = 0; ks < 4; ++ks) {
                u4 wf = wv[q * 4 + ks];
                a0 = mfma16(vf[0][ks], wf, a0);
                a1 = mfma16(vf[1][ks], wf, a1);
            }
            int ct = (c - 24) * 4 + q;
            st16<SC1>(ubase, ct * 2 * 16384 + uoff, pack8(a0, a1));
        }
        if (c < 31) { asm volatile("s_waitcnt vmcnt(%0)" :: "n"(c == 24 ? 24 : 4 * (30 - c) + 4) : "memory"); RAW_BARRIER(); }
    }
    RING_SYNC(0);
    if (tid == 0) st4_sc1(p.ws + O_FLG, F_A + sg * 256 + h * 4, 1u);
}
DEVI void unitAp(const Params& p, char* smem, int sg, int h, int part) {
    const int tid = threadIdx.x, l = tid & 63, w = __builtin_amdgcn_readfirstlane(tid >> 6), g = l >> 4, m = l & 15;
    const int s = sg * 4 + w, c0 = part < 2 ? 8 * part : 16;
    RAW_BARRIER();
    u4 xf[2][16];
#pragma unroll
    for (int a = 0; a < 2; ++a)
#pragma unroll
        for (int kk = 0; kk < 16; ++kk) xf[a][kk] = *(const u4*)(p.ws + O_XT + ((size_t)(s * 2 + a) * 16 + kk) * 1024 + l * 16);
#pragma unroll
    for (int c = 0; c < 8; ++c) ring_fill(p, smem, h, c0 + c, w, l);
    u4 vf[2][4];
#pragma unroll
    for (int pr = 0; pr < 4; ++pr) {
        if (pr == 0) RING_SYNC(16);
        if (pr == 2) {
            RING_SYNC(0);
            if (part >= 2) {
#pragma unroll
                for (int c = 0; c < 4; ++c) ring_fill(p, smem - (part - 2) * 65536, h, 24 + (part - 2) * 4 + c, w, l);
            }
        }
        f4 e0, e1, o0, o1;
        projtile(smem + (2 * pr) * 16384, xf, e0, e1, l);
        projtile(smem + (2 * pr + 1) * 16384, xf, o0, o1, l);
        vf[0][pr] = pack8(e0, o0); vf[1][pr] = pack8(e1, o1);
    }
    if (part < 2) {
        char* dst = p.ws + (part == 0 ? O_QX : O_KX) + (size_t)(s * 32 + h) * 8192;
#pragma unroll
        for (int pr = 0; pr < 4; ++pr) { st16_sc1(dst, pr * 1024 + l * 16, vf[0][pr]); st16_sc1(dst, 4096 + pr * 1024 + l * 16, vf[1][pr]); }
    } else {
        RING_SYNC(0);
        char* ubase = p.ws + O_UX + (size_t)s * 64 * 16384 + (size_t)((h >> 3) * 4 + ((h >> 1) & 3)) * 1024;
        const int uoff = (m >> 3) * 16384 + (g * 16 + (h & 1) * 8 + (m & 7)) * 16;
        for (int c = 0; c < 4; ++c) {
            u4 wv[16];
#pragma unroll
            for (int kk = 0; kk < 16; ++kk) wv[kk] = *(const u4*)(smem + c * 16384 + kk * 1024 + l * 16);
            __builtin_amdgcn_sched_barrier(0);
#pragma unroll
            for (int q = 0; q < 4; ++q) {
                f4 a0 = {0, 0, 0, 0}, a1 = {0, 0, 0, 0};
#pragma unroll
                for (int ks = 0; ks < 4; ++ks) { a0 = mfma16(vf[0][ks], wv[q * 4 + ks], a0); a1 = mfma16(vf[1][ks], wv[q * 4 + ks], a1); }
                st16_sc1(ubase, (((part - 2) * 4 + c) * 4 + q) * 2 * 16384 + uoff, pack8(a0, a1));
            }
        }
    }
    RING_SYNC(0);
    if (tid == 0) st4_sc1(p.ws + O_FLG, F_AP + sg * 512 + h * 16 + part * 4, 1u);
}
constexpr int S4_RC = 4416;
constexpr int S4_KH = 4 * S4_RC;
constexpr int S4_KL = S4_KH + 5200;
constexpr int S4_KD = S4_KL + 5200;
constexpr int S4_WV = S4_KD + 2688 + 16;
constexpr int S4_Y = 4 * S4_WV;
constexpr int S4_LDS = S4_Y + 16384;
static_assert(S4_WV % 16 == 0 && S4_KH % 16 == 0 && S4_KL % 16 == 0, "align");
DEVI void cexpl(float ar, float ai, float l, float& er, float& ei) {
    float mag = __builtin_amdgcn_exp2f(ar * l * 1.44269504088896f);
    float rev = __builtin_amdgcn_fractf(ai * l * 0.15915494309189535f);
    er = mag * __builtin_amdgcn_cosf(rev); ei = mag * __builtin_amdgcn_sinf(rev);
}
DEVI void csq(float& r, float& i) { float nr = r * r - i * i; i = 2.f * r * i; r = nr; }
DEVI void unitS4(const Params& p, char* smem, int dq) {
    const int tid = threadIdx.x, l = tid & 63, w = __builtin_amdgcn_readfirstlane(tid >> 6);
    const int d = dq * 4 + w;
    char* wb = smem + w * S4_WV;
    RAW_BARRIER();
    for (int t = tid; t < 2048; t += 256) {
        u2 v = *(const u2*)(p.x + (size_t)t * 512 + dq * 4);
        int x = 2047 - t;
#pragma unroll
        for (int ch = 0; ch < 4; ++ch) {
            u16 e = (u16)((ch & 1) ? (v[ch >> 1] >> 16) : v[ch >> 1]);
#pragma unroll
            for (int rho = 0; rho < 4; ++rho) if (x - rho >= 0) *(u16*)(smem + ch * S4_WV + rho * S4_RC + (x - rho) * 2) = e;
        }
    }
    if (l < 48) {
#pragma unroll
        for (int rho = 0; rho < 4; ++rho) *(u16*)(wb + rho * S4_RC + (2048 - rho + l) * 2) = 0;
    }
    if (l < 20) { *(unsigned*)(wb + S4_KH + l * 4) = 0; *(unsigned*)(wb + S4_KL + l * 4) = 0; }
    {
        const float dt = rbf(__expf(bf2f(p.logdt[d])));
        const int idx = l & 15, kq = l >> 4;
        f4 acc[4] = {};
        for (int it = 0; it < 32; ++it) {
            int n = d * 128 + it * 4 + kq;
            float ar = dt * bf2f(p.are[n]), ai = dt * bf2f(p.aim[n]);
            float br = dt * bf2f(p.bre[n]), bi = dt * bf2f(p.bim[n]);
            float cr = bf2f(p.cre[n]), ci = bf2f(p.cim[n]);
            float kr = cr * br - ci * bi, ki = cr * bi + ci * br;
            float lr, li, h0r, h0i, h1r, h1i;
            cexpl(ar, ai, (float)idx, lr, li);
            cexpl(ar, ai, (float)(16 * idx), h0r, h0i);
            cexpl(ar, ai, (float)(256 + 32 * idx), h1r, h1i);
#pragma unroll
            for (int r = 0; r < 4; ++r) {
                float hr = r == 0 ? h0r : h1r, hi = r == 0 ? h0i : h1i;
                float xr = hr * kr - hi * ki, xi = hr * ki + hi * kr;
                acc[r] = __builtin_amdgcn_mfma_f32_16x16x4f32(xr, lr, acc[r], 0, 0, 0);
                acc[r] = __builtin_amdgcn_mfma_f32_16x16x4f32(-xi, li, acc[r], 0, 0, 0);
                if (r < 3) csq(lr, li);
                if (r >= 1 && r < 3) csq(h1r, h1i);
            }
        }
        float* kd = (float*)(wb + S4_KD);
#pragma unroll
        for (int r = 0; r < 4; ++r)
#pragma unroll
            for (int rr = 0; rr < 4; ++rr) {
                int a = 4 * kq + rr;
                bool ok = r == 0 || a < 8 || (r == 3 && a == 8);
                if (ok) kd[(r == 0 ? 0 : 128 + 128 * r) + 16 * a + idx] = acc[r][rr];
            }
        asm volatile("s_waitcnt lgkmcnt(0)" ::: "memory");
        for (int i = 0; i < 16; ++i) {
            int l0 = (i * 64 + l) * 2;
            float kv[2];
#pragma unroll
            for (int q = 0; q < 2; ++q) {
                int lb = (int)rbf((float)(l0 + q));
                int ix = lb < 256 ? lb : lb < 512 ? 256 + ((lb - 256) >> 1) : lb < 1024 ? 384 + ((lb - 512) >> 2) : 512 + ((lb - 1024) >> 3);
                kv[q] = kd[ix];
            }
            float h0 = rbf(kv[0]), h1 = rbf(kv[1]);
            int o = ((l0 >> 5) + 1) * 80 + (l0 & 31) * 2;
            *(unsigned*)(wb + S4_KH + o) = pk2(h0, h1);
            *(unsigned*)(wb + S4_KL + o) = pk2(kv[0] - h0, kv[1] - h1);
        }
    }
    RAW_BARRIER();
    {
        const int b = l & 31, hh = l >> 5;
        const int x00 = 2047 - b + 8 * hh;
        const char* abase = wb + (x00 & 3) * S4_RC + (x00 & ~3) * 2;
        f16v acc0 = {}, acc1 = {};
        for (int dl = 0; dl < 32; ++dl) {
            int r0 = b - dl; r0 = r0 < -1 ? -1 : r0;
            const char* b0 = wb + S4_KH + (r0 + 1) * 80 + hh * 16;
            const char* b1 = wb + S4_KH + (32 + b - dl + 1) * 80 + hh * 16;
#pragma unroll
            for (int ks = 0; ks < 2; ++ks) {
                int th = 2 * dl - ks;
                const char* ap = abase - th * 32;
                u2 a0l = *(const u2*)(ap), a0h = *(const u2*)(ap + 8);
                u2 a1l = *(const u2*)(ap - 2048), a1h = *(const u2*)(ap - 2048 + 8);
                u4 A0 = {a0l.x, a0l.y, a0h.x, a0h.y}, A1 = {a1l.x, a1l.y, a1h.x, a1h.y};
                u4 B0h = *(const u4*)(b0 + ks * 32), B0l = *(const u4*)(b0 + ks * 32 + 5200);
                u4 B1h = *(const u4*)(b1 + ks * 32), B1l = *(const u4*)(b1 + ks * 32 + 5200);
                acc0 = mfma32(A0, B0h, acc0); acc1 = mfma32(A0, B1h, acc1);
                acc0 = mfma32(A0, B0l, acc0); acc1 = mfma32(A0, B1l, acc1);
                acc1 = mfma32(A1, B0h, acc1); acc1 = mfma32(A1, B0l, acc1);
            }
        }
        const float dsk = bf2f(p.dskip[d]);
#pragma unroll
        for (int tau = 0; tau < 2; ++tau)
#pragma unroll
            for (int r = 0; r < 16; ++r) {
                int t = 32 * (32 * tau + b) + (r & 3) + 8 * (r >> 2) + 4 * hh;
                float y = rbf(tau ? acc1[r] : acc0[r]);
                float u = bf2f(*(const u16*)(wb + (2047 - t) * 2));
                *(u16*)(smem + S4_Y + t * 8 + w * 2) = f2bf(y + rbf(u * dsk));
            }
    }
    RAW_BARRIER();
    for (int t = tid; t < 2048; t += 256) st8_sc1(p.ws + O_B, t * 1024 + dq * 8, *(const u2*)(smem + S4_Y + t * 8));
    RING_SYNC(0);
    if (tid == 0) st4_sc1(p.ws + O_FLG, F_S4 + dq * 4, 1u);
}

#define ST(k)
#define ST_FLUSH(slot)
DEVI void poll2(const char* base, int off, u4& v0, u4& v1) {
    for (unsigned spins = 0; spins < SPIN_CAP; ++spins) {
        asm volatile("s_nop 4\n\tglobal_load_dwordx4 %0, %2, %3 sc1\n\tglobal_load_dwordx4 %1, %2, %3 offset:1024 sc1\n\ts_waitcnt vmcnt(0)"
                     : "=&v"(v0), "=&v"(v1) : "v"(off), "s"(base) : "memory");
        if (__builtin_amdgcn_ballot_w64(mx4(mx4(0u, v0), v1) == SENT) == 0) break;
    }
}
DEVI void poll5(const char* base, int offx, int offm, u4 (&v)[5]) {
    for (unsigned spins = 0; spins < SPIN_CAP; ++spins) {
        asm volatile("s_nop 4\n\tglobal_load_dwordx4 %0, %5, %7 sc1\n\tglobal_load_dwordx4 %1, %5, %7 offset:1024 sc1\n\t"
                     "global_load_dwordx4 %2, %5, %7 offset:2048 sc1\n\tglobal_load_dwordx4 %3, %5, %7 offset:3072 sc1\n\t"
                     "global_load_dwordx4 %4, %6, %7 sc1\n\ts_waitcnt vmcnt(0)"
                     : "=&v"(v[0]), "=&v"(v[1]), "=&v"(v[2]), "=&v"(v[3]), "=&v"(v[4]) : "v"(offx), "v"(offm), "s"(base) : "memory");
        unsigned m = mx4(mx4(mx4(mx4(mx4(0u, v[0]), v[1]), v[2]), v[3]), v[4]);
        if (__builtin_amdgcn_ballot_w64(m == SENT) == 0) break;
    }
}
#define SMF4(A, I, PRE) asm volatile(PRE "v_smfmac_f32_16x16x64_bf16 %0, %4, %5, %9\n\tv_smfmac_f32_16x16x64_bf16 %1, %4, %6, %9\n\t" \
                     "v_smfmac_f32_16x16x64_bf16 %2, %4, %7, %9\n\tv_smfmac_f32_16x16x64_bf16 %3, %4, %8, %9" \
                     : "+v"(acc[0]), "+v"(acc[1]), "+v"(acc[2]), "+v"(acc[3]) \
                     : "v"(A), "a"(wt[0][I]), "a"(wt[1][I]), "a"(wt[2][I]), "a"(wt[3][I]), "v"(sidx))
DEVI void mem_project(char* smem, int s, const char* memg, char* lmem, const u8v (&wt)[4][8], f4 (&acc)[4], int w, int l, int sidx) {
    u4 v0, v1;
#pragma unroll
    for (int t = 0; t < 4; ++t) { acc[t] = f4{0, 0, 0, 0}; asm volatile("" : "+v"(acc[t])); }
    poll2(memg, w * 2048 + l * 16, v0, v1);
    ST(0);
    *(u4*)(lmem + w * 2048 + l * 16) = v0; *(u4*)(lmem + w * 2048 + 1024 + l * 16) = v1;
    __builtin_amdgcn_sched_barrier(0);
    SMF4(v0, 0, "");
    RAW_BARRIER();
    ST(1);
    u4 a[8];
#pragma unroll
    for (int i = 2; i < 8; ++i) a[i] = *(const u4*)(lmem + ((2 * w + i) & 7) * 1024 + l * 16);
    __builtin_amdgcn_sched_barrier(0);
    SMF4(v1, 1, "");
    SMF4(a[2], 2, ""); SMF4(a[3], 3, ""); SMF4(a[4], 4, ""); SMF4(a[5], 5, ""); SMF4(a[6], 6, ""); SMF4(a[7], 7, "");
    asm volatile("s_nop 7" : "+v"(acc[0]), "+v"(acc[1]), "+v"(acc[2]), "+v"(acc[3]));
    ST(2);
}
DEVI u8v ldw(const char* p) { u4 a = *(const u4*)p, b = *(const u4*)(p + 1024); u8v r = {a.x, a.y, a.z, a.w, b.x, b.y, b.z, b.w}; return r; }
#define TIE8(T) asm volatile("" : "+a"(wt[T][0]), "+a"(wt[T][1]), "+a"(wt[T][2]), "+a"(wt[T][3]), "+a"(wt[T][4]), "+a"(wt[T][5]), "+a"(wt[T][6]), "+a"(wt[T][7]))
DEVI void score12(f4& c0, f4& c1, f4& c2, const u4 (&a0)[4], const u4 (&a1)[4], const u4 (&a2)[4], const u4 (&b)[4]) {
    asm volatile("v_mfma_f32_16x16x32_bf16 %0, %3, %6, 0\n\tv_mfma_f32_16x16x32_bf16 %1, %4, %6, 0\n\tv_mfma_f32_16x16x32_bf16 %2, %5, %6, 0"
                 : "=&v"(c0), "=&v"(c1), "=&v"(c2) : "v"(a0[0]), "v"(a1[0]), "v"(a2[0]), "v"(b[0]));
#pragma unroll
    for (int ks = 1; ks < 4; ++ks)
        asm volatile("v_mfma_f32_16x16x32_bf16 %0, %3, %6, %0\n\tv_mfma_f32_16x16x32_bf16 %1, %4, %6, %1\n\tv_mfma_f32_16x16x32_bf16 %2, %5, %6, %2"
                     : "+v"(c0), "+v"(c1), "+v"(c2) : "v"(a0[ks]), "v"(a1[ks]), "v"(a2[ks]), "v"(b[ks]));
    asm volatile("s_nop 7" : "+v"(c0), "+v"(c1), "+v"(c2));
}
DEVI void trfrags(unsigned addr, u4 (&f)[4]) {
    u2 t[8];
    asm volatile("ds_read_b64_tr_b16 %0, %8\n\tds_read_b64_tr_b16 %1, %8 offset:512\n\tds_read_b64_tr_b16 %2, %8 offset:1024\n\tds_read_b64_tr_b16 %3, %8 offset:1536\n\t"
                 "ds_read_b64_tr_b16 %4, %8 offset:2048\n\tds_read_b64_tr_b16 %5, %8 offset:2560\n\tds_read_b64_tr_b16 %6, %8 offset:3072\n\tds_read_b64_tr_b16 %7, %8 offset:3584"
                 : "=&v"(t[0]), "=&v"(t[1]), "=&v"(t[2]), "=&v"(t[3]), "=&v"(t[4]), "=&v"(t[5]), "=&v"(t[6]), "=&v"(t[7]) : "v"(addr) : "memory");
    asm volatile("s_waitcnt lgkmcnt(0)" : "+v"(t[0]), "+v"(t[1]), "+v"(t[2]), "+v"(t[3]), "+v"(t[4]), "+v"(t[5]), "+v"(t[6]), "+v"(t[7]) :: "memory");
#pragma unroll
    for (int ks = 0; ks < 4; ++ks) f[ks] = u4{t[2 * ks].x, t[2 * ks].y, t[2 * ks + 1].x, t[2 * ks + 1].y};
}
DEVI void score_mem(unsigned addr, f4& c0, f4& c1, f4& c2, const u4 (&a0)[4], const u4 (&a1)[4]) {
    asm volatile("ds_read_b64_tr_b16 v[120:121], %3 offset:0\n\t"
                 "ds_read_b64_tr_b16 v[122:123], %3 offset:512\n\t"
                 "ds_read_b64_tr_b16 v[124:125], %3 offset:1024\n\t"
                 "ds_read_b64_tr_b16 v[126:127], %3 offset:1536\n\t"
                 "ds_read_b64_tr_b16 v[128:129], %3 offset:2048\n\t"
                 "ds_read_b64_tr_b16 v[130:131], %3 offset:2560\n\t"
                 "ds_read_b64_tr_b16 v[132:133], %3 offset:3072\n\t"
                 "ds_read_b64_tr_b16 v[134:135], %3 offset:3584\n\t"
                 "ds_read_b64_tr_b16 v[136:137], %3 offset:4096\n\t"
                 "ds_read_b64_tr_b16 v[138:139], %3 offset:4608\n\t"
                 "ds_read_b64_tr_b16 v[140:141], %3 offset:5120\n\t"
                 "ds_read_b64_tr_b16 v[142:143], %3 offset:5632\n\t"
                 "ds_read_b64_tr_b16 v[144:145], %3 offset:6144\n\t"
                 "ds_read_b64_tr_b16 v[146:147], %3 offset:6656\n\t"
                 "ds_read_b64_tr_b16 v[148:149], %3 offset:7168\n\t"
                 "ds_read_b64_tr_b16 v[150:151], %3 offset:7680\n\t"
                 "s_waitcnt lgkmcnt(14)\n\t"
                 "v_mfma_f32_16x16x32_bf16 %0, %4, v[120:123], 0\n\t"
                 "v_mfma_f32_16x16x32_bf16 %1, %8, v[120:123], 0\n\t"
                 "s_waitcnt lgkmcnt(12)\n\t"
                 "v_mfma_f32_16x16x32_bf16 %0, %5, v[124:127], %0\n\t"
                 "v_mfma_f32_16x16x32_bf16 %1, %9, v[124:127], %1\n\t"
                 "s_waitcnt lgkmcnt(6)\n\t"
                 "v_mfma_f32_16x16x32_bf16 %2, v[136:139], v[120:123], 0\n\t"
                 "v_mfma_f32_16x16x32_bf16 %0, %6, v[128:131], %0\n\t"
                 "s_waitcnt lgkmcnt(4)\n\t"
                 "v_mfma_f32_16x16x32_bf16 %2, v[140:143], v[124:127], %2\n\t"
                 "v_mfma_f32_16x16x32_bf16 %1, %10, v[128:131], %1\n\t"
                 "s_waitcnt lgkmcnt(2)\n\t"
                 "v_mfma_f32_16x16x32_bf16 %2, v[144:147], v[128:131], %2\n\t"
                 "v_mfma_f32_16x16x32_bf16 %0, %7, v[132:135], %0\n\t"
                 "s_waitcnt lgkmcnt(0)\n\t"
                 "v_mfma_f32_16x16x32_bf16 %2, v[148:151], v[132:135], %2\n\t"
                 "v_mfma_f32_16x16x32_bf16 %1, %11, v[132:135], %1\n\t"
                 "s_nop 7\n\t"
                 : "=&v"(c0), "=&v"(c1), "=&v"(c2) : "v"(addr), "v"(a0[0]), "v"(a0[1]), "v"(a0[2]), "v"(a0[3]), "v"(a1[0]), "v"(a1[1]), "v"(a1[2]), "v"(a1[3])
                 : "memory", "v120", "v121", "v122", "v123", "v124", "v125", "v126", "v127", "v128", "v129", "v130", "v131", "v132", "v133", "v134", "v135", "v136", "v137", "v138", "v139", "v140", "v141", "v142", "v143", "v144", "v145", "v146", "v147", "v148", "v149", "v150", "v151");
}
DEVI float xmax(float v) {
    float t;
    asm volatile("s_nop 1\n\tv_max_f32_dpp %0, %0, %0 row_ror:8 row_mask:0xf bank_mask:0xf\n\tv_mov_b32 %1, %0\n\ts_nop 1\n\t"
                 "v_permlane32_swap_b32 %0, %1\n\tv_max_f32 %0, %0, %1\n\tv_mov_b32 %1, %0\n\ts_nop 1\n\t"
                 "v_permlane16_swap_b32 %0, %1\n\tv_max_f32 %0, %0, %1" : "+v"(v), "=&v"(t));
    return v;
}
DEVI float ror8(float v) { return __builtin_bit_cast(float, __builtin_amdgcn_update_dpp(0, __builtin_bit_cast(int, v), 0x128, 0xF, 0xF, false)); }
DEVI void ld4_sc1(const char* base, int off, u4 (&v)[4]) {
    asm volatile("s_nop 4\n\tglobal_load_dwordx4 %0, %4, %5 sc1\n\tglobal_load_dwordx4 %1, %4, %5 offset:1024 sc1\n\t"
                 "global_load_dwordx4 %2, %4, %5 offset:2048 sc1\n\tglobal_load_dwordx4 %3, %4, %5 offset:3072 sc1\n\ts_waitcnt vmcnt(0)"
                 : "=&v"(v[0]), "=&v"(v[1]), "=&v"(v[2]), "=&v"(v[3]) : "v"(off), "s"(base) : "memory");
}
DEVI void ld12_sc1(const char* kb, const char* qb, int off, u4 (&a)[4], u4 (&b)[4], u4 (&c)[4]) {
    asm volatile("s_nop 4\n\tglobal_load_dwordx4 %0, %12, %13 offset:-4096 sc1\n\tglobal_load_dwordx4 %1, %12, %13 offset:-3072 sc1\n\t"
                 "global_load_dwordx4 %2, %12, %13 offset:-2048 sc1\n\tglobal_load_dwordx4 %3, %12, %13 offset:-1024 sc1\n\t"
                 "global_load_dwordx4 %4, %12, %13 sc1\n\tglobal_load_dwordx4 %5, %12, %13 offset:1024 sc1\n\t"
                 "global_load_dwordx4 %6, %12, %13 offset:2048 sc1\n\tglobal_load_dwordx4 %7, %12, %13 offset:3072 sc1\n\t"
                 "global_load_dwordx4 %8, %12, %14 offset:-4096 sc1\n\tglobal_load_dwordx4 %9, %12, %14 offset:-3072 sc1\n\t"
                 "global_load_dwordx4 %10, %12, %14 offset:-2048 sc1\n\tglobal_load_dwordx4 %11, %12, %14 offset:-1024 sc1\n\ts_waitcnt vmcnt(0)"
                 : "=&v"(a[0]), "=&v"(a[1]), "=&v"(a[2]), "=&v"(a[3]), "=&v"(b[0]), "=&v"(b[1]), "=&v"(b[2]), "=&v"(b[3]),
                   "=&v"(c[0]), "=&v"(c[1]), "=&v"(c[2]), "=&v"(c[3]) : "v"(off), "s"(kb), "s"(qb) : "memory");
}

DEVI void role_score(const Params& p, char* smem, int h) {
    const int tid = threadIdx.x, l = tid & 63, w = __builtin_amdgcn_readfirstlane(tid >> 6), g = l >> 4, m = l & 15;
    char* lmem = smem; char* lq = smem + 8192;
    u8v wt[4][8];
#pragma unroll
    for (int t = 0; t < 4; ++t) {
#pragma unroll
        for (int i = 0; i < 8; ++i)
            wt[t][i] = ldw(p.ws + O_WT + ((size_t)((t >> 1) * 32 + h) * 8 + 2 * w + (t & 1)) * 16384 + ((2 * w + i) & 7) * 2048 + l * 16);
        if (t == 1) { __builtin_amdgcn_sched_barrier(0); TIE8(0); TIE8(1); }
        if (t == 3) { __builtin_amdgcn_sched_barrier(0); TIE8(2); TIE8(3); }
    }
    const int sidx = (l & 8) ? 0xEEEE : 0x4444;
    const bool hi8 = (l & 8) != 0;
    char* wq = lq + (32 * w + m) * 32 + g * 8;
    const unsigned traddr = (unsigned)(size_t)(LAS char*)lq + (4 * g + (m >> 2)) * 32 + (m & 3) * 8;
    const float sc = 0.08838834764831845f * 1.44269504088896f;
    for (int s = 0; s < NSEG; ++s) {
        u4 kx0[4], kx1[4], qx[4];
        if (w < 3) {
            if ((s & 3) == 0) wait_words<2>(p.ws + O_FLG, s < 4 * NSPLIT ? F_AP + (s >> 2) * 512 + h * 16 + (l & 1) * 4 : F_A + (s >> 2) * 256 + h * 4);
            ld12_sc1(p.ws + O_KX + (size_t)(s * 32 + h) * 8192, p.ws + O_QX + (size_t)(s * 32 + h) * 8192 + (w >> 1) * 4096, l * 16 + 4096, kx0, kx1, qx);
        }
        f4 acc[4];
        mem_project(smem, s, p.ws + O_MEM + s * MEMSTEP, lmem, wt, acc, w, l, sidx);
#pragma unroll
        for (int t = 0; t < 4; ++t) *(u2*)(wq + (t >> 1) * 4096 + (t & 1) * 512) = u2{pk2(acc[t][0], acc[t][1]), pk2(acc[t][2], acc[t][3])};
        RAW_BARRIER();
        ST(3);
        f4 s0, s1, sm;
        if (w == 0) {
            if (s < NSEG - 1) {
                score_mem(traddr, s0, s1, sm, kx0, kx1);
                float v[5];
#pragma unroll
                for (int r = 0; r < 4; ++r) v[r] = (hi8 ? s1[r] : s0[r]) + ror8(hi8 ? s0[r] : s1[r]);
                float ma = swap32_add(sm[0], sm[1]), mb = swap32_add(sm[2], sm[3]);
                v[4] = (hi8 ? mb : ma) + ror8(hi8 ? ma : mb);
                float mxv = fmaxf(fmaxf(fmaxf(v[0], v[1]), fmaxf(v[2], v[3])), v[4]);
                mxv = xmax(mxv);
#pragma unroll
                for (int r = 0; r < 5; ++r) v[r] = __builtin_amdgcn_exp2f((v[r] - mxv) * sc);
                float sum = ((v[0] + v[1]) + (v[2] + v[3])) + v[4];
                sum += ror8(sum);
                float inv = __builtin_amdgcn_rcpf(gsum(sum));
                u2 px = {nosent(pk2(v[0] * inv, v[1] * inv)), nosent(pk2(v[2] * inv, v[3] * inv))};
                float pm0 = v[4] * inv;
                auto r2 = __builtin_amdgcn_permlane32_swap(__float_as_uint(pm0), __float_as_uint(pm0), false, false);
                unsigned pmv = nosent(pk2(__uint_as_float(r2[0]), __uint_as_float(r2[1])));
                char* pb = p.ws + O_PM + s * (PMSTEP * NREP);
                asm volatile("s_nop 4\n\tglobal_store_dwordx2 %0, %1, %4 sc1\n\tglobal_store_dword %2, %3, %4 sc1\n\ts_nop 1"
                             :: "v"(h * 512 + g * 128 + (l & 7) * 16 + (l & 8)), "v"(px), "v"(16384 + h * 128 + (l & 7) * 16 + (g & 1) * 8 + (l & 8) / 2), "v"(pmv), "s"(pb) : "memory");
                ST(4);
            }
        } else if (w < 3) {
            __builtin_amdgcn_s_sleep(6);
            u4 km[4];
            trfrags(traddr + 4096, km);
            score12(s0, s1, sm, kx0, kx1, km, qx);
            const int qi = 8 * (m >> 2) + 4 * (w - 1) + (m & 3);
            float v[12];
#pragma unroll
            for (int r = 0; r < 4; ++r) {
                float t = swap32_add(sm[r], sm[r]);
                v[r] = g < 2 ? t * sc : -INFINITY;
                v[4 + r] = (8 * g + r <= qi) ? s0[r] * sc : -INFINITY;
                v[8 + r] = (8 * g + 4 + r <= qi) ? s1[r] * sc : -INFINITY;
            }
            float mxv = v[0];
#pragma unroll
            for (int i = 1; i < 12; ++i) mxv = fmaxf(mxv, v[i]);
            mxv = gmax(mxv);
            float sum = 0.f;
#pragma unroll
            for (int i = 0; i < 12; ++i) { v[i] = __builtin_amdgcn_exp2f(v[i] - mxv); sum += v[i]; }
            float inv = __builtin_amdgcn_rcpf(gsum(sum));
            u4 px = {pk2(v[4] * inv, v[5] * inv), pk2(v[6] * inv, v[7] * inv), pk2(v[8] * inv, v[9] * inv), pk2(v[10] * inv, v[11] * inv)};
            u2 pmv = {pk2(v[0] * inv, v[1] * inv), pk2(v[2] * inv, v[3] * inv)};
            st16_sc1(p.ws + O_PXX + (size_t)((s * 2 + w - 1) * 32 + h) * 1024, l * 16, px);
            if (g < 2) st8_sc1(p.ws + O_PXM + (size_t)((s * 2 + w - 1) * 32 + h) * 256, m * 16 + g * 8, pmv);
            VM0();
            if (l == 0) st4_sc1(p.ws + O_FLG, F_PX + s * 256 + (h * 2 + w - 1) * 4, 1u);
            ST(4);
        }
    }
    ST_FLUSH(h);
}
DEVI void role_col(const Params& p, char* smem, int cb) {
    const int tid = threadIdx.x, l = tid & 63, w = __builtin_amdgcn_readfirstlane(tid >> 6), g = l >> 4, m = l & 15;
    char* lmem = smem; char* lz = smem + 8192 + w * 1024; char* lred = smem + 12288;
    u8v wt[4][8];
#pragma unroll
    for (int t = 0; t < 4; ++t) {
#pragma unroll
        for (int i = 0; i < 8; ++i)
            wt[t][i] = ldw(p.ws + O_FT + (((size_t)(cb * 4 + w) * 4 + t) * 8 + ((2 * w + i) & 7)) * 2048 + l * 16);
        if (t == 1) { __builtin_amdgcn_sched_barrier(0); TIE8(0); TIE8(1); }
        if (t == 3) { __builtin_amdgcn_sched_barrier(0); TIE8(2); TIE8(3); }
    }
    const int sidx = (l & 8) ? 0xEEEE : 0x4444;
    const int kh = (l >> 3) & 1, tok = l & 7;
    const int rep = NREP == 2 ? (cb >> 5) * PMSTEP : 0;
    const int poffx = rep + (8 * w + kh) * 512 + g * 128 + tok * 16;
    const int poffm = rep + 16384 + (8 * w + 4 * kh + g) * 128 + tok * 16;
    for (int s = 0; s < NSEG; ++s) {
        u4 uf[4];
        if ((s & 3) == 0) wait_words<2>(p.ws + O_FLG, s < 4 * NSPLIT ? F_AP + (s >> 2) * 512 + (l & 31) * 16 + 8 + (cb >> 5) * 4 : F_A + (s >> 2) * 256 + (l & 31) * 4);
        ld4_sc1(p.ws + O_UX + ((size_t)(s * 64 + cb) * 4 + w) * 4096, l * 16, uf);
        f4 acc[4];
        mem_project(smem, s, p.ws + O_MEM2 + s * MEMSTEP, lmem, wt, acc, w, l, sidx);
#pragma unroll
        for (int pr = 0; pr < 2; ++pr) {
            float z[4];
#pragma unroll
            for (int r = 0; r < 4; ++r) z[r] = swap32_add(acc[2 * pr][r], acc[2 * pr + 1][r]);
            int t = 2 * pr + (l >> 5);
            *(u2*)(lz + ((2 * t + (m >> 3)) * 8 + (m & 7)) * 16 + (g & 1) * 8) = u2{pk2(z[0], z[1]), pk2(z[2], z[3])};
        }
        asm volatile("s_waitcnt lgkmcnt(0)" ::: "memory");
        u4 zf = *(const u4*)(lz + ((4 * kh + g) * 8 + tok) * 16);
        st16_sc1(p.ws + O_ZM + ((size_t)(s * 64 + cb) * 4 + w) * 1024, l * 16, zf);
        ST(3);
        if (s == NSEG - 1) {
            RING_SYNC(0);
            if (tid == 64) st4_sc1(p.ws + O_FLG, F_Z + s * 256 + cb * 4, 1u);
            break;
        }
        u4 pf[5];
        poll5(p.ws + O_PM + s * (PMSTEP * NREP), poffx, poffm, pf);
        ST(4);
        f4 d0;
        asm volatile("v_mfma_f32_16x16x32_bf16 %0, %1, %6, 0\n\tv_mfma_f32_16x16x32_bf16 %0, %2, %7, %0\n\tv_mfma_f32_16x16x32_bf16 %0, %3, %8, %0\n\t"
                     "v_mfma_f32_16x16x32_bf16 %0, %4, %9, %0\n\tv_mfma_f32_16x16x32_bf16 %0, %5, %10, %0\n\ts_nop 7"
                     : "=&v"(d0) : "v"(uf[0]), "v"(uf[1]), "v"(uf[2]), "v"(uf[3]), "v"(zf), "v"(pf[0]), "v"(pf[1]), "v"(pf[2]), "v"(pf[3]), "v"(pf[4]));
        if ((l >> 5) == kh) *(f4*)(lred + ((w * 2 + kh) * 16 + tok * 2 + (g & 1)) * 16) = d0;
        RAW_BARRIER();
        ST(5);
        if (tid == 64) st4_sc1(p.ws + O_FLG, F_Z + s * 256 + cb * 4, 1u);
        if (w == 0) {
            typedef float f2 __attribute__((ext_vector_type(2)));
            f2 t[8];
#pragma unroll
            for (int i = 0; i < 8; ++i) t[i] = *(const f2*)(lred + i * 256 + (l & 31) * 8);
            __builtin_amdgcn_sched_barrier(0);
            f2 a = ((t[0] + t[1]) + (t[2] + t[3])) + ((t[4] + t[5]) + (t[6] + t[7]));
            st4_sc1(p.ws + O_MEM + (s + 1) * MEMSTEP, (l >> 5) * (int)(O_MEM2 - O_MEM) + cb * 128 + (l & 31) * 4, nosent(pk2(a[0], a[1])));
        }
        ST(6);
    }
    ST_FLUSH(32 + cb);
}

constexpr int RED_RS = 272;
DEVI void unitOut(const Params& p, char* smem, int s, int cblk) {
    const int tid = threadIdx.x, l = tid & 63, w = __builtin_amdgcn_readfirstlane(tid >> 6), g = l >> 4, m = l & 15;
    f4 acc[4][2] = {};
    wait_words<8>(p.ws + O_FLG, l < 32 ? (s < 4 * NSPLIT ? F_AP + (s >> 2) * 512 + l * 16 + 8 + (cblk >> 2) * 4 : F_A + (s >> 2) * 256 + l * 4) : F_S4 + (cblk * 16 + (l & 15)) * 4);
    __builtin_amdgcn_fence(__ATOMIC_ACQUIRE, "agent");
    const int aoff = (m >> 3) * 16384 + (g * 16 + (m & 7)) * 16;
    const char* ub = p.ws + O_UX + ((size_t)(s * 64 + cblk * 8) * 4 + w) * 4096 + aoff;
    u4 ua[8][4];
#pragma unroll
    for (int hh = 0; hh < 8; ++hh)
#pragma unroll
        for (int ct = 0; ct < 4; ++ct) ua[hh][ct] = *(const u4*)(ub + ct * 32768 + (hh >> 1) * 1024 + (hh & 1) * 128);
    const int tok = tid >> 3, cg = tid & 7;
    const int t = s * 32 + tok;
    const float gv = ((const float*)(p.ws + O_G))[t], og = rbf(1.f - gv);
    u4 bv = *(const u4*)(p.ws + O_B + (size_t)t * 1024 + cblk * 128 + cg * 16);
    wait_words<2>(p.ws + O_FLG, F_Z + s * 256 + (cblk * 8 + (l & 7)) * 4);
#pragma unroll
    for (int hh = 0; hh < 4; ++hh) asm volatile("" : "+a"(ua[hh][0]), "+a"(ua[hh][1]), "+a"(ua[hh][2]), "+a"(ua[hh][3]));
    wait_words<2>(p.ws + O_FLG, F_PX + s * 256 + l * 4);
    __builtin_amdgcn_fence(__ATOMIC_ACQUIRE, "agent");
    const char* pxx = p.ws + O_PXX + (size_t)(s * 64 + 8 * w) * 1024 + l * 16;
    const char* zb = p.ws + O_ZM + ((size_t)(s * 64 + cblk * 8) * 4 + w) * 1024 + (m >> 3) * 4096 + (g * 16 + (m & 7)) * 16;
    const char* pxm = p.ws + O_PXM + (size_t)(s * 64 + 8 * w) * 256 + l * 16;
    u4 pb[8][2], pm[2][2], za[2][4];
#pragma unroll
    for (int hh = 0; hh < 8; ++hh) { pb[hh][0] = *(const u4*)(pxx + hh * 1024); pb[hh][1] = *(const u4*)(pxx + 32768 + hh * 1024); }
#pragma unroll
    for (int kh = 0; kh < 2; ++kh) {
        pm[kh][0] = *(const u4*)(pxm + kh * 1024); pm[kh][1] = *(const u4*)(pxm + 8192 + kh * 1024);
#pragma unroll
        for (int ct = 0; ct < 4; ++ct) za[kh][ct] = *(const u4*)(zb + ct * 8192 + kh * 128);
    }
    __builtin_amdgcn_sched_barrier(0);
#pragma unroll
    for (int hh = 0; hh < 8; ++hh)
#pragma unroll
        for (int ct = 0; ct < 4; ++ct) { acc[ct][0] = mfma16(ua[hh][ct], pb[hh][0], acc[ct][0]); acc[ct][1] = mfma16(ua[hh][ct], pb[hh][1], acc[ct][1]); }
#pragma unroll
    for (int kh = 0; kh < 2; ++kh)
#pragma unroll
        for (int ct = 0; ct < 4; ++ct) { acc[ct][0] = mfma16(za[kh][ct], pm[kh][0], acc[ct][0]); acc[ct][1] = mfma16(za[kh][ct], pm[kh][1], acc[ct][1]); }
    RAW_BARRIER();
#pragma unroll
    for (int a = 0; a < 2; ++a)
#pragma unroll
        for (int ct = 0; ct < 4; ++ct)
            *(f4*)(smem + (w * 32 + 8 * (m >> 2) + 4 * a + (m & 3)) * RED_RS + (16 * ct + 4 * g) * 4) = acc[ct][a];
    RAW_BARRIER();
    f4 r0 = {0, 0, 0, 0}, r1 = {0, 0, 0, 0};
#pragma unroll
    for (int ww = 0; ww < 4; ++ww) {
        r0 += *(const f4*)(smem + (ww * 32 + tok) * RED_RS + cg * 32);
        r1 += *(const f4*)(smem + (ww * 32 + tok) * RED_RS + cg * 32 + 16);
    }
    unsigned o[4];
#pragma unroll
    for (int j = 0; j < 4; ++j) {
        float a0 = j < 2 ? r0[2 * j] : r1[2 * j - 4], a1 = j < 2 ? r0[2 * j + 1] : r1[2 * j - 3];
        float b0 = __uint_as_float(bv[j] << 16), b1 = __uint_as_float(bv[j] & 0xFFFF0000u);
        o[j] = pk2(rbf(gv * rbf(a0)) + rbf(og * b0), rbf(gv * rbf(a1)) + rbf(og * b1));
    }
    *(u4*)(p.out + (size_t)t * 512 + cblk * 64 + cg * 8) = u4{o[0], o[1], o[2], o[3]};
}
constexpr int LDS_BYTES = 144 * 1024;
__global__ void __launch_bounds__(256, 1) k_main(Params p) {
    extern __shared__ __attribute__((aligned(16))) char smem[];
    const int x = blockIdx.x & 7, r = blockIdx.x >> 3;
#define BST()
    BST();
    if (x == 0) { role_score(p, smem, r); return; }
    if (x <= 2) { role_col(p, smem, (x - 1) * 32 + r); return; }
    const int j = r * 5 + (x - 3);
    const int xb = x - 3, nh = xb < 2 ? 7 : 6;
    for (int u = r; u < (16 + 3 * NSPLIT) * nh; u += 32) {
        if (u < 4 * NSPLIT * nh) { int q = u / nh; unitAp(p, smem, q >> 2, xb + 5 * (u % nh), 3 - (q & 3)); }
        else unitA<true>(p, smem, u / nh - 3 * NSPLIT, xb + 5 * (u % nh));
        if (u < 128) BST();
    }
    if (xb >= 2 || r >= 16) unitS4(p, smem, xb >= 2 ? (xb - 2) * 32 + r : 80 + xb * 16 + r);
    BST();
    for (int u = j; u < 512; u += 160) { unitOut(p, smem, u >> 3, u & 7); BST(); }
}

extern "C" void kernel_launch(void* const* d_in, const int* in_sizes, int n_in, void* d_out, int out_size, void* d_ws, size_t ws_size, hipStream_t stream) {
    Params p;
    p.x = (const u16*)d_in[0]; p.mem0 = (const u16*)d_in[1]; p.wq = (const u16*)d_in[2]; p.wk = (const u16*)d_in[3];
    p.wv = (const u16*)d_in[4]; p.wo = (const u16*)d_in[5];
    p.are = (const u16*)d_in[8]; p.aim = (const u16*)d_in[9]; p.bre = (const u16*)d_in[10]; p.bim = (const u16*)d_in[11];
    p.cre = (const u16*)d_in[12]; p.cim = (const u16*)d_in[13]; p.logdt = (const u16*)d_in[14]; p.dskip = (const u16*)d_in[15];
    p.gw = (const u16*)d_in[16];
    p.out = (u16*)d_out; p.ws = (char*)d_ws;
    hipFuncSetAttribute((const void*)k_main, hipFuncAttributeMaxDynamicSharedMemorySize, LDS_BYTES);
    k_pre<<<PRE_F + PRE_G + PRE_S + PRE_RT, 256, 0, stream>>>(p);
    k_main<<<256, 256, LDS_BYTES, stream>>>(p);
}
```
